```python
import torch

def _softmax(x, axis=-1):
    return torch.softmax(x, dim=axis)
def _gelu(x):
    return torch.nn.functional.gelu(x, approximate="tanh")
def _silu(x):
    return torch.nn.functional.silu(x)
def _sigmoid(x):
    return torch.sigmoid(x)
def _layernorm(x, axis=-1, eps=1e-5):
    m = x.mean(dim=axis, keepdim=True)
    v = x.var(dim=axis, keepdim=True, unbiased=False)
    return (x - m) / torch.sqrt(v + eps)
def _scatter_add(tgt, idx, src, axis=0):
    return tgt.clone().index_add_(axis, idx, src)
def _segment_sum(src, seg, n):
    out = torch.zeros((n,) + src.shape[1:], dtype=src.dtype)
    return out.index_add_(0, seg, src)
def _pad(x, pw):
    flat = tuple(v for pair in reversed(pw) for v in pair)
    return torch.nn.functional.pad(x, flat)

kernel_name = 'parallel_sum__gqa_block_cached__mixer_channel_mlp__decode'

def setup_inputs():

    _w = (lambda a: (lambda t: t.to(torch.bfloat16) if t.is_floating_point() else t)(torch.as_tensor(a)))
    def _A_setup():
        B, Sq, D = 4, 1, 1024
        Hq, Hkv, Sk, Dh = 32, 4, 16384, 128
        assert Hq % Hkv == 0, (Hq, Hkv)
        return {
            "x": _w((lambda *s: torch.randn(*s, dtype=torch.bfloat16) * 0.05)(B, Sq, D)),
            "k_cache": _w((lambda *s: torch.randn(*s, dtype=torch.bfloat16) * 3.2)(B, Hkv, Sk, Dh)),
            "v_cache": _w((lambda *s: torch.randn(*s, dtype=torch.bfloat16) * 0.4)(B, Hkv, Sk, Dh)),
            "wq": _w((lambda *s: torch.randn(*s, dtype=torch.bfloat16) * 3.2)(D, Hq * Dh) * (D ** -0.5)),
            "wo": _w((lambda *s: torch.randn(*s, dtype=torch.bfloat16) * 0.4)(Hq * Dh, D) * ((Hq * Dh) ** -0.5)),
        }
    _A_in = _A_setup()

    def _B_setup():
        B, S, D, F = 4, 1, 1024, 3584
        return {
            "x": _w((lambda *s: torch.randn(*s, dtype=torch.bfloat16) * 0.05)(B, S, D)),
            "w1": _w((lambda *s: torch.randn(*s, dtype=torch.bfloat16) * 3.2)(D, F) * (D ** -0.5)),
            "b1": _w((lambda *s: torch.randn(*s, dtype=torch.bfloat16) * 1.6)(F)),
            "w2": _w((lambda *s: torch.randn(*s, dtype=torch.bfloat16) * 0.1)(F, D) * (F ** -0.5)),
            "b2": _w((lambda *s: torch.randn(*s, dtype=torch.bfloat16) * 0.1)(D)),
        }
    _B_in = _B_setup()

    return {
        "A_x": _A_in["x"],
        "A_k_cache": _A_in["k_cache"],
        "A_v_cache": _A_in["v_cache"],
        "A_wq": _A_in["wq"],
        "A_wo": _A_in["wo"],
        "B_x": _B_in["x"],
        "B_w1": _B_in["w1"],
        "B_b1": _B_in["b1"],
        "B_w2": _B_in["w2"],
        "B_b2": _B_in["b2"],
    }

def reference(A_x, A_k_cache, A_v_cache, A_wq, A_wo, B_x, B_w1, B_b1, B_w2, B_b2):
    def _A_ref(x, k_cache, v_cache, wq, wo):
        B, Sq, D = x.shape
        Hq, Dh = 32, 128
        Hkv = k_cache.shape[1]
        rep = Hq // Hkv
        q = (lambda x, *d: x.permute(*d))((x @ wq).reshape(B, Sq, Hq, Dh), 0, 2, 1, 3)
        k = (lambda x, n, axis: torch.repeat_interleave(x, n, dim=axis))(k_cache, rep, axis=1)
        v = (lambda x, n, axis: torch.repeat_interleave(x, n, dim=axis))(v_cache, rep, axis=1)
        scores = torch.einsum("bhqd,bhkd->bhqk", q, k) * (Dh ** -0.5)
        p = _softmax(scores, axis=-1)
        o = torch.einsum("bhqk,bhkd->bhqd", p, v)
        return (lambda x, *d: x.permute(*d))(o, 0, 2, 1, 3).reshape(B, Sq, Hq * Dh) @ wo

    def _B_ref(x, w1, b1, w2, b2):
        y = _layernorm(x, axis=-1)
        h = _gelu(torch.einsum("bsd,df->bsf", y, w1) + b1)
        return torch.einsum("bsf,fd->bsd", h, w2) + b2

    x = A_x
    xn = _layernorm(x, axis=-1)
    return x + _A_ref(xn, A_k_cache, A_v_cache, A_wq, A_wo) + _B_ref(xn, B_w1, B_b1, B_w2, B_b2)
```

```cpp
#include <hip/hip_runtime.h>
#include <hip/hip_bf16.h>

typedef unsigned short u16;
typedef unsigned v4u __attribute__((ext_vector_type(4)));
typedef unsigned v2u __attribute__((ext_vector_type(2)));
typedef float f32x4 __attribute__((ext_vector_type(4)));
typedef float f32x2 __attribute__((ext_vector_type(2)));
typedef short bf16x8 __attribute__((ext_vector_type(8)));
typedef __amdgpu_buffer_rsrc_t rsrc_t;
#define LAS __attribute__((address_space(3)))

constexpr int NB = 4, DM = 1024, SK = 16384, DH = 128, FF = 3584, NQ = 4096;
constexpr int NWG = 256, NTHR = 768;
constexpr int SC1 = 16, NT = 2;

constexpr int WFA = 2, WLO = 3, WIN = 5, WINL = 6;
constexpr int TAU = 272;
constexpr int C1 = 440, C2 = 1465;
constexpr int I1 = 14, I2 = 49, SW2 = 62;
constexpr int MA = 30, MB = 80, SLA = 6, SLB = 6;
constexpr int MA2 = 20, MB2 = 35;
constexpr int MAL = -15, MBL = 55, TAUL = 255;
constexpr int QGO = 19, QPOS = 27;
constexpr int GT = 2225;
constexpr int NLP = 3;
static_assert(WLO == 3 && QGO > WLO && QPOS <= 28 && QGO < QPOS && WLO <= WIN && NLP <= WIN, "schedule");

constexpr int WS_EPOCH = 0, WS_T1 = 256, WS_T2 = 512;
constexpr int WS_QG = 4096;
constexpr int WS_HG = WS_QG + 2 * 4 * NQ * 4;
constexpr int WS_SG = WS_HG + 2 * 4 * FF * 4;
constexpr int WS_OG = WS_SG + 16 * 16 * 64;
constexpr int OG_STR = 32768, PG_STR = 4096;
constexpr int WS_PG = WS_OG + 32 * OG_STR;
constexpr int WS_END = WS_PG + 128 * PG_STR;
constexpr int XN_STR = 2048 + 16, QS_STR = 256 + 16, R_STR = 132 * 4, E_STR = 68 * 4;
constexpr int L_XN = 0;
constexpr int L_QS = L_XN + 4 * XN_STR;
constexpr int L_WS = L_QS + 8 * QS_STR;
constexpr int L_FAC = L_WS + 8 * 8 * 8;
constexpr int L_IN = L_FAC + 8 * 8 * 4;
constexpr int L_ZERO = L_IN + 8 * 4 * 64;
constexpr int F_XN = L_ZERO + 16, F_Q = F_XN + 16, F_EARLY = F_Q + 16, F_STAT = F_EARLY + 32, F_GO = F_STAT + 32, F_O = F_GO + 16,
              F_MID = F_O + 32, L_REF = F_MID + 16;
constexpr int L_RED = L_REF + 16;
constexpr int L_RED2 = L_RED + 64 * R_STR;
constexpr int L_END = L_RED2 + 32 * R_STR;
constexpr int LDS_BYTES = 96 * 1024;
static_assert(L_END + 8192 <= LDS_BYTES && L_RED % 16 == 0 && L_RED - L_ZERO == 12 * 16, "lds");
constexpr unsigned SPIN_CAP = 1u << 18;

__device__ __forceinline__ float bflo(unsigned x) { return __builtin_bit_cast(float, x << 16); }
__device__ __forceinline__ float bfhi(unsigned x) { return __builtin_bit_cast(float, x & 0xffff0000u); }
__device__ __forceinline__ unsigned pack2(float lo, float hi) {
  typedef __bf16 bf2 __attribute__((ext_vector_type(2)));
  const bf2 r = __builtin_convertvector(f32x2{lo, hi}, bf2);
  return __builtin_bit_cast(unsigned, r);
}
__device__ __forceinline__ float rbf(float f) { return bflo(pack2(f, f)); }
__device__ __forceinline__ float asf(unsigned x) { return __builtin_bit_cast(float, x); }
__device__ __forceinline__ unsigned asu(float x) { return __builtin_bit_cast(unsigned, x); }
__device__ __forceinline__ f32x4 asf4(v4u x) { return __builtin_bit_cast(f32x4, x); }
template <int CTRL> __device__ __forceinline__ float dpp(float x) {
  return __builtin_bit_cast(float, __builtin_amdgcn_mov_dpp(__builtin_bit_cast(int, x), CTRL, 0xf, 0xf, true));
}
constexpr int XOR1 = 0xB1, XOR2 = 0x4E, XOR7 = 0x141, XOR15 = 0x140, XOR8 = 0x128, ROR4 = 0x124;
__device__ __forceinline__ float sc16(float a, float b) { auto s = __builtin_amdgcn_permlane16_swap(asu(a), asu(b), false, false); return asf(s[0]) + asf(s[1]); }
__device__ __forceinline__ float sc32(float a, float b) { auto s = __builtin_amdgcn_permlane32_swap(asu(a), asu(b), false, false); return asf(s[0]) + asf(s[1]); }
__device__ __forceinline__ float scatter4(f32x4 s) { return sc16(sc32(s[0], s[2]), sc32(s[1], s[3])); }
__device__ __forceinline__ float x16max(float x) { auto s = __builtin_amdgcn_permlane16_swap(asu(x), asu(x), false, false); return fmaxf(asf(s[0]), asf(s[1])); }
__device__ __forceinline__ float x32max(float x) { auto s = __builtin_amdgcn_permlane32_swap(asu(x), asu(x), false, false); return fmaxf(asf(s[0]), asf(s[1])); }
__device__ __forceinline__ float sum_hi2(float v) { v = sc16(v, v); return sc32(v, v); }
__device__ __forceinline__ float sum_hi3(float v) { v += dpp<XOR8>(v); return sum_hi2(v); }
__device__ __forceinline__ float max_hi3(float v) { v = fmaxf(v, dpp<XOR8>(v)); return x32max(x16max(v)); }
__device__ __forceinline__ float wave_sum(float v) {
  v += dpp<XOR1>(v); v += dpp<XOR2>(v); v += dpp<XOR7>(v); v += dpp<XOR15>(v);
  return sum_hi2(v);
}
__device__ __forceinline__ float expd(float a, float b) { return __builtin_amdgcn_exp2f((a - b) * 1.4426950408889634f); }
__device__ __forceinline__ f32x4 mfma(v4u a, v4u b, f32x4 c) {
  return __builtin_amdgcn_mfma_f32_16x16x32_bf16(__builtin_bit_cast(bf16x8, a), __builtin_bit_cast(bf16x8, b), c, 0, 0, 0);
}
template <int AUX> __device__ __forceinline__ v4u ld16(rsrc_t r, int voff, int soff) { return __builtin_amdgcn_raw_buffer_load_b128(r, voff, soff, AUX); }
template <int N> __device__ __forceinline__ void waitvm() { asm volatile("s_waitcnt vmcnt(%0)" ::"i"(N) : "memory"); }
__device__ __forceinline__ void bar() { asm volatile("s_waitcnt lgkmcnt(0)\n\ts_barrier" ::: "memory"); }
#define PIN() do { asm volatile("" ::: "memory"); __builtin_amdgcn_sched_barrier(0); } while (0)
#define KEEP(x) asm volatile("" : "+v"(x))
__device__ __forceinline__ rsrc_t mkrs(const void* p, int bytes) { return __builtin_amdgcn_make_buffer_rsrc((void*)p, 0, bytes, 0x00020000); }
__device__ __forceinline__ unsigned clk() { return (unsigned)__builtin_amdgcn_s_memrealtime(); }
#define THROTTLE(now, tref, TA, TB, NA_, NB_) \
  asm volatile("s_sub_u32 vcc_lo, %0, %1\n\ts_cmpk_gt_i32 vcc_lo, %3\n\ts_cbranch_scc1 1f\n\ts_waitcnt vmcnt(%5)\n\t" \
               "s_cmpk_gt_i32 vcc_lo, %2\n\ts_cbranch_scc1 1f\n\ts_waitcnt vmcnt(%4)\n1:" \
               ::"s"(now), "s"(tref), "i"(TA), "i"(TB), "i"((NA_) - 1), "i"((NB_) - 1) : "vcc", "scc", "memory")

__device__ __forceinline__ unsigned next_epoch(unsigned e) { return ((e & 0x7f7f7f7fu) + 0x07050301u) ^ (e & 0x80808080u); }
__device__ __forceinline__ v4u tags_of(unsigned e) { return v4u{e & 255u, (e >> 8) & 255u, (e >> 16) & 255u, e >> 24}; }
__device__ __forceinline__ void publish4(f32x4 v, v4u tag, rsrc_t r, int voff, int soff) {
  __builtin_amdgcn_raw_buffer_store_b128((__builtin_bit_cast(v4u, v) & 0xffffff00u) | tag, r, voff, soff, SC1);
}
template <int N> __device__ __forceinline__ void request(v4u (&g)[N], rsrc_t rs, int voff, int soff, int stride) {
#pragma unroll
  for (int i = 0; i < N; ++i) g[i] = ld16<SC1>(rs, voff, soff + i * stride);
}
template <int N> __device__ __forceinline__ f32x4 strip_sum(v4u (&g)[N], v4u tag, unsigned& any) {
  any = 0;
#pragma unroll
  for (int i = 0; i < N; ++i) { g[i] ^= tag; any |= (g[i][0] | g[i][1]) | (g[i][2] | g[i][3]); }
  any &= 255u;
  if constexpr (N == 4) return (asf4(g[0]) + asf4(g[1])) + (asf4(g[2]) + asf4(g[3]));
  else if constexpr (N == 2) return asf4(g[0]) + asf4(g[1]);
  else return asf4(g[0]);
}
template <int N, int SLEEP> __device__ __forceinline__ f32x4 settle(v4u (&g)[N], rsrc_t rs, int voff, int soff, int stride, v4u tag) {
  unsigned any;
  f32x4 s = strip_sum<N>(g, tag, any);
  if (__builtin_amdgcn_ballot_w64(any != 0) != 0) {
    unsigned spins = SPIN_CAP;
#pragma nounroll
    do {
      if (SLEEP) __builtin_amdgcn_s_sleep(SLEEP);
      asm volatile("" ::: "memory");
      any |= asu(dpp<XOR1>(asf(any))); any |= asu(dpp<XOR2>(asf(any)));
      if (any != 0) {
        v4u t[N];
        request<N>(t, rs, voff, soff, stride);
        s = strip_sum<N>(t, tag, any);
      }
    } while (__builtin_amdgcn_ballot_w64(any != 0) != 0 && --spins);
  }
  return s;
}
__device__ __forceinline__ void sample_put(unsigned t, unsigned epoch, rsrc_t rs, int off, int wg, int lane) {
  if (((wg + 16) >> 5) == 4 && lane == 0) __builtin_amdgcn_raw_buffer_store_b64(v2u{t, t ^ epoch ^ 0x5bd1e995u}, rs, 0, off + (wg & 31) * 8, SC1);
}
__device__ __forceinline__ unsigned sample_ref(unsigned epoch, rsrc_t rs, int off, int lane) {
  const unsigned now = clk();
  const v2u t = __builtin_amdgcn_raw_buffer_load_b64(rs, (lane & 31) * 8, off, SC1);
  const float d = (t[0] ^ t[1] ^ 0x5bd1e995u) == epoch ? (float)(int)(t[0] - now) : 0.f;
  const float mean = wave_sum(d) * (1.f / 64);
  return now + (int)(mean + wave_sum(fabsf(d - mean)) * (8 / 256.f));
}
__device__ __forceinline__ void flag_set(LAS unsigned char* p) { asm volatile("" ::: "memory"); *(volatile LAS unsigned*)p = 1u; }
template <int N4, int SLEEP> __device__ __forceinline__ void flag_wait(LAS unsigned char* p) {
#pragma nounroll
  for (unsigned spins = 0; spins < (SPIN_CAP << 4); ++spins) {
    unsigned a = 1u;
#pragma unroll
    for (int i = 0; i < N4; ++i) { const v4u f = *(volatile LAS v4u*)(p + 16 * i); a &= f[0] & f[1] & f[2] & f[3]; }
    if (__builtin_amdgcn_readfirstlane(a)) break;
    __builtin_amdgcn_s_sleep(SLEEP);
  }
  asm volatile("" ::: "memory");
}

__device__ __forceinline__ void ln16(float (&v)[16]) {
  float s = 0.f;
#pragma unroll
  for (int i = 0; i < 16; i += 4) s += (v[i] + v[i + 1]) + (v[i + 2] + v[i + 3]);
  const float mean = wave_sum(s) * (1.f / DM);
  float q0 = 0.f, q1 = 0.f;
#pragma unroll
  for (int i = 0; i < 16; i += 2) { q0 = fmaf(v[i] - mean, v[i] - mean, q0); q1 = fmaf(v[i + 1] - mean, v[i + 1] - mean, q1); }
  const float var = wave_sum(q0 + q1) * (1.f / DM);
  const float mb = rbf(mean), inv = 1.f / rbf(sqrtf(rbf(rbf(var) + 1e-5f)));
#pragma unroll
  for (int i = 0; i < 16; i += 2) {
    const unsigned d = pack2(v[i] - mb, v[i + 1] - mb);
    const unsigned o = pack2(bflo(d) * inv, bfhi(d) * inv);
    v[i] = bflo(o); v[i + 1] = bfhi(o);
  }
}
template <int P> __device__ __forceinline__ void trp(v4u r0, v4u r1, v4u (&f)[8]) {
  if (P == 0) asm volatile("" : "=v"(f[0]), "=v"(f[1]), "=v"(f[2]), "=v"(f[3]), "=v"(f[4]), "=v"(f[5]), "=v"(f[6]), "=v"(f[7]));
#pragma unroll
  for (int j = 0; j < 8; ++j) f[j][P] = __builtin_amdgcn_perm(r1[j >> 1], r0[j >> 1], (j & 1) ? 0x07060302u : 0x05040100u);
  asm volatile("" : "+v"(f[0]), "+v"(f[1]), "+v"(f[2]), "+v"(f[3]), "+v"(f[4]), "+v"(f[5]), "+v"(f[6]), "+v"(f[7]));
}
__device__ __forceinline__ void late_unit(const v4u (&f)[8], LAS unsigned char* L, const int (&ao)[4], int paddr) {
  v4u act[4];
#pragma unroll
  for (int q = 0; q < 4; ++q) act[q] = *(const LAS v4u*)(L + ao[q]);
  __builtin_amdgcn_sched_barrier(0);
  f32x4 a0 = {0.f, 0.f, 0.f, 0.f}, a1 = a0;
#pragma unroll
  for (int q = 0; q < 4; ++q) { a0 = mfma(f[2 * q], act[q], a0); a1 = mfma(f[2 * q + 1], act[q], a1); }
  *(LAS f32x4*)(L + paddr) = a0; *(LAS f32x4*)(L + paddr + 16) = a1;
}

__global__ void __launch_bounds__(NTHR, 3)
fused_kernel(const u16* x, const u16* Kc, const u16* Vc, const u16* wq, const u16* w1, const u16* wo, const u16* w2, unsigned char* ws,
             const u16* b1, const u16* b2, u16* out) {
  extern __shared__ __attribute__((aligned(16))) unsigned char smem[];
  LAS unsigned char* L = (LAS unsigned char*)smem;
  typedef unsigned long long u64x8 __attribute__((ext_vector_type(8)));
  typedef unsigned long long u64x2 __attribute__((ext_vector_type(2)));
  u64x8 ka; u64x2 kb; unsigned long long kc;
  int wg = blockIdx.x;
  asm volatile("s_load_dwordx16 %0, %4, 0x0\n\ts_load_dwordx4 %1, %4, 0x40\n\ts_load_dwordx2 %2, %4, 0x50"
               : "=&s"(ka), "=&s"(kb), "=&s"(kc), "+s"(wg) : "s"(__builtin_amdgcn_kernarg_segment_ptr()));
  __builtin_amdgcn_sched_barrier(0);
  const int tid = threadIdx.x, lane = tid & 63, wv = __builtin_amdgcn_readfirstlane(tid >> 6), g = lane >> 4, c = lane & 15;
  const bool early = wg < 240, isq = wg < 128;
  const int eid = isq ? wg : early ? wg - 128 : 0, ldw = isq ? NQ * 2 : FF * 2;
  const int kh = (eid >> 2) & 1, ct = (eid >> 3) * 4 + (eid & 3);
  int evo = (lane >> 3) * ldw + (c & 7) * 16;
  const int eso = (512 * kh + 64 * ((wv + 4) & 7)) * ldw + ct * 128, estr = 8 * ldw;
  int ebytes = early && wv >= 4 ? DM * ldw : 0;
  const int ex = ((wv + 4) & 7) ^ ((wv & 1) * 4);
  int e0 = eso + ex * estr, e1 = eso + (ex ^ 1) * estr, e2 = eso + (ex ^ 2) * estr;
  asm volatile("" : "+v"(evo));
  asm volatile("" : "+s"(e0), "+s"(e1), "+s"(e2), "+s"(ebytes));
  __builtin_amdgcn_sched_barrier(0);
  asm volatile("s_waitcnt lgkmcnt(0)" : "+s"(ka), "+s"(kb), "+s"(kc));
  x = (const u16*)ka[0]; Kc = (const u16*)ka[1]; Vc = (const u16*)ka[2]; wq = (const u16*)ka[3]; w1 = (const u16*)ka[4]; wo = (const u16*)ka[5];
  w2 = (const u16*)ka[6]; ws = (unsigned char*)ka[7]; b1 = (const u16*)kb[0]; b2 = (const u16*)kb[1]; out = (u16*)kc;
  v4u wr[8];
  const rsrc_t re = mkrs(isq ? wq : w1, ebytes);
#define LDE(j) wr[j] = ld16<NT>(re, evo, eso + ((j) ^ ex) * estr)
  wr[0] = ld16<NT>(re, evo, e0); wr[1] = ld16<NT>(re, evo, e1); wr[2] = ld16<NT>(re, evo, e2);
  PIN();
  asm volatile("" : "+s"(wg));
  const int grp = (wg & 7) * 2 + ((wg >> 3) & 1), sp = wg >> 4;
  const int bb = grp >> 2, hk = grp & 3;
  const int ks = (wg & 7) * 4 + (wg >> 6), cg = (wg >> 3) & 7;
  const rsrc_t rws = mkrs(ws, WS_END);

  if (wv >= 4) {
    const int w = wv - 4;
    v4u kv[32], fr[8];
    __builtin_amdgcn_s_setprio(1);
    asm volatile("s_barrier" ::: "memory");
    const rsrc_t rk = mkrs(Kc + ((size_t)grp * SK + sp * 1024) * DH, 1024 * DH * 2);
    const rsrc_t rv = mkrs(Vc + ((size_t)grp * SK + sp * 1024) * DH, 1024 * DH * 2);
    const int rev = (w & 1) * 2048 + (w >> 2) * 1024, hfx = ((w ^ (w >> 1)) & 1) * 128;
    const int wso = w * 4096 + (w & 1) * 131072;
    int ko[4], vo[4];
#pragma unroll
    for (int i = 0; i < 4; ++i) ko[i] = ((4 * ((c >> 1) & 1) + (c >> 2)) * 256 + g * 32 + (c & 1) * 16 + (i & 1) * 128 + (i & 2) * 1024) ^ (rev + hfx);
#define LDK(n) kv[n] = ld16<NT>(rk, ko[(n) & 3], wso ^ (((n) >> 2) * 32768))
#define LDV(n) kv[n] = ld16<NT>(rv, vo[(n) & 3], wso ^ (((n) >> 2) * 32768))
#define SLOT0(ld) do { waitvm<WLO - 1>(); ld; PIN(); } while (0)
#define DRIFT(i) ((i) - ((i) < SW2 ? I1 : I2))
#define SLOT(i, ld) do { waitvm<WIN - 1>(); THROTTLE(now, tref, ((i) * TAU - DRIFT(i) * SLA) / 10 - ((i) < SW2 ? MA : MA2), ((i) * TAU + DRIFT(i) * SLB) / 10 + ((i) < SW2 ? MB : MB2), WFA, WLO); \
      ld; PIN(); now = clk(); PIN(); } while (0)
#define SLOTL(j, ld) do { waitvm<WINL - 1>(); THROTTLE(now, tref, (72 * TAU + (j) * TAUL) / 10 - MAL, (72 * TAU + (j) * TAUL) / 10 + MBL, WFA, WLO); \
      ld; PIN(); now = clk(); PIN(); } while (0)
#define TRP(p, r0, r1) do { if ((p) == 0) trp<0>(r0, r1, fr); else if ((p) == 1) trp<1>(r0, r1, fr); else if ((p) == 2) trp<2>(r0, r1, fr); else trp<3>(r0, r1, fr); PIN(); } while (0)
#pragma unroll
    for (int i = WLO; i <= 8 + WLO; ++i) {
      if (i < 8) SLOT0(LDE(i)); else SLOT0(LDK(i - 8));
      if (((i - WLO) & 1) && i - WLO < 8) TRP((i - WLO) >> 1, wr[i - WLO - 1], wr[i - WLO]);
    }
    flag_wait<1, 1>(L + F_XN);
    {
      const int ao = L_XN + (c & 3) * XN_STR + (512 * kh + 64 * w + 16 * g + 8 * ((c >> 2) & 1)) * 2;
      const v4u a0 = *(const LAS v4u*)(L + (c < 8 ? ao : L_ZERO)), a1 = *(const LAS v4u*)(L + (c < 8 ? L_ZERO : ao));
      f32x4 a[4];
#pragma unroll
      for (int j = 0; j < 4; ++j) a[j] = mfma(fr[j], a0, f32x4{0.f, 0.f, 0.f, 0.f});
#pragma unroll
      for (int j = 0; j < 4; ++j) a[j] = mfma(fr[4 + j], a1, a[j]);
      if (((c >> 2) & 1) == (g >> 1)) {
#pragma unroll
        for (int r = 0; r < 4; ++r)
          *(LAS f32x4*)(L + L_RED + ((w * 2 + (g >> 1)) * 4 + (c & 3)) * E_STR + (g & 1) * 128 + r * 32 + (c >> 3) * 16) = f32x4{a[0][r], a[1][r], a[2][r], a[3][r]};
      }
      flag_set(L + F_EARLY + w * 4);
    }
    PIN();
#pragma unroll
    for (int n = WLO + 1; n <= QPOS; ++n) {
      SLOT0(LDK(n));
      if (n == QGO) { flag_set(L + F_GO + (w & 3) * 4); PIN(); }
    }
    flag_wait<1, 1>(L + F_Q);
    v4u qf[2];
#pragma unroll
    for (int s = 0; s < 2; ++s) qf[s] = *(const LAS v4u*)(L + L_QS + (c & 7) * QS_STR + ((s * 128) ^ hfx) + g * 32 + (c >> 3) * 16);
#pragma unroll
    for (int i = 0; i < 4; ++i) vo[i] = (lane * 16 + i * 1024) ^ rev;
    const v2u ref = *(const LAS v2u*)(L + L_REF);
    unsigned tref = __builtin_amdgcn_readfirstlane(ref[0]), now = clk();
    const unsigned epoch = __builtin_amdgcn_readfirstlane(ref[1]);
    const bool par = c >= 8;
    const float scale = 0.08838834764831845f;
    float S[16], m = asf(0xff7f0000u);
#define QK2(t) do { _Pragma("unroll") for (int t_ = (t); t_ < (t) + 2; ++t_) { \
      const f32x4 d_ = mfma(kv[2 * t_ + 1], qf[1], mfma(kv[2 * t_], qf[0], f32x4{0.f, 0.f, 0.f, 0.f})); \
      S[t_] = (par ? d_[3] : d_[0]) + dpp<XOR8>(par ? d_[1] : d_[2]); } \
    const unsigned a_ = pack2(S[t], S[(t) + 1]), s_ = pack2(bflo(a_) * scale, bfhi(a_) * scale); \
    S[t] = bflo(s_); S[(t) + 1] = bfhi(s_); m = fmaxf(m, fmaxf(S[t], S[(t) + 1])); \
    asm volatile("" : "+v"(S[t]), "+v"(S[(t) + 1]), "+v"(m)); PIN(); } while (0)
    {
      int done = 1;
      QK2(0);
#pragma unroll
      for (int n = QPOS + 1; n < 32 + WIN; ++n) {
        if (n < 32) SLOT(8 + n, LDK(n)); else SLOT(8 + n, LDV(n - 32));
        if (done < 8 && 4 * done + 3 <= n - WIN) { QK2(2 * done); ++done; }
      }
#pragma unroll
      for (int rep = 0; rep < 8; ++rep)
        if (done < 8) { QK2(2 * done); ++done; }
    }
    m = max_hi3(m);
    v4u Plo[4], Phi[4];
    float l = 0.f;
    f32x4 O[4];
#pragma unroll
    for (int j = 0; j < 4; ++j) O[j] = f32x4{0.f, 0.f, 0.f, 0.f};
    const bool iswo = w < 4;
    const int u = w & 3;
    const rsrc_t rl = mkrs(iswo ? wo : w2, iswo ? NQ * DM * 2 : FF * DM * 2);
    const rsrc_t rl7 = mkrs(iswo ? wo : w2, iswo ? NQ * DM * 2 : 0);
    const int lvo = g * (iswo ? 2 : 7) * 2048 + c * 16;
    const int lso = (iswo ? u * 1024 + ks * 32 : ks * 112 + u * 28) * 2048 + cg * 256;
#define LDL(j) wr[j] = ld16<NT>((j) == 7 ? rl7 : rl, lvo, lso + (iswo ? 8 * ((j) >> 1) + ((j) & 1) : ((j) < 7 ? (j) : 6)) * 2048)
    unsigned tnext = 0;
    int oaddr = L_RED + (w * 8 + (c & 7)) * R_STR + g * 128 + (c >> 3) * 64;
    KEEP(oaddr);
#pragma unroll
    for (int p = WIN; p < 32 + WIN; ++p) {
      if (40 + p == SW2 - 1 || 40 + p == 68) tnext = *(const volatile LAS unsigned*)(L + L_REF);
      if (40 + p == SW2 || 40 + p == 69) tref = __builtin_amdgcn_readfirstlane(tnext);
      if (p < 32) SLOT(40 + p, LDV(p));
      else if (p < 32 + NLP) SLOTL(p - 32, LDL(p - 32));
      const int n = p - WIN;
      if (n < 4) {
#pragma unroll
        for (int i = 4 * n; i < 4 * n + 4; i += 2) {
          const float p0 = expd(S[i], m), p1 = expd(S[i + 1], m);
          l += p0 + p1;
          const unsigned own = pack2(p0, p1), oth = asu(dpp<XOR8>(asf(own)));
          Plo[n][i & 3] = par ? 0u : __builtin_amdgcn_perm(oth, own, 0x05040100u);
          Plo[n][(i & 3) + 1] = par ? 0u : __builtin_amdgcn_perm(oth, own, 0x07060302u);
          Phi[n][i & 3] = par ? __builtin_amdgcn_perm(own, oth, 0x05040100u) : 0u;
          Phi[n][(i & 3) + 1] = par ? __builtin_amdgcn_perm(own, oth, 0x07060302u) : 0u;
        }
        asm volatile("" : "+v"(Plo[n & 3]), "+v"(Phi[n & 3]), "+v"(l));
        PIN();
      }
      if (n == 4) {
        l = sum_hi3(l);
        if (lane < 8) *(LAS f32x2*)(L + L_WS + (w * 8 + lane) * 8) = f32x2{m, l};
        flag_set(L + F_STAT + w * 4);
        PIN();
      }
      if (n & 1) {
        TRP((n & 7) >> 1, kv[n - 1], kv[n]);
        if ((n & 7) == 7) {
          const int T = n >> 3;
#pragma unroll
          for (int j = 0; j < 4; ++j) O[j] = mfma(fr[j], Plo[T], O[j]);
#pragma unroll
          for (int j = 0; j < 4; ++j) O[j] = mfma(fr[4 + j], Phi[T], O[j]);
          if (T < 3) { asm volatile("" : "+v"(O[0]), "+v"(O[1]), "+v"(O[2]), "+v"(O[3])); PIN(); }
        }
      }
    }
#pragma unroll
    for (int j = 0; j < 4; ++j) *(LAS f32x4*)(L + oaddr + j * 16) = O[j];
    flag_set(L + F_O + w * 4);
    PIN();
#pragma unroll
    for (int j = NLP; j < 8; ++j) SLOTL(j, LDL(j));
    int ao[4], iaddr = L_IN + (w * 2 + (c >> 3)) * 64 + (c & 6) * 8 + g * 4 + (c & 1) * 2, paddr = L_RED2 + (w * 4 + (c & 3)) * R_STR + g * 128 + (c >> 2) * 32;
#pragma unroll
    for (int q = 0; q < 4; ++q) { ao[q] = (c >> 2) == q ? L_IN + (w * 4 + (c & 3)) * 64 + g * 16 : L_ZERO; KEEP(ao[q]); }
    int ovo = (c >> 3) * 2048 + g * 128 + (c & 7) * 16;
    KEEP(iaddr); KEEP(paddr); KEEP(ovo);
    flag_wait<1, 1>(L + F_MID);
    v4u og[4];
    const int oso = WS_OG + ks * OG_STR + w * 4096;
    v4u tag = tags_of(epoch);
    asm volatile("" : "+s"(tag));
    THROTTLE(now, tref, GT - 40, GT, 2, 3);
    request<4>(og, rws, ovo, oso, 512);
    PIN();
#pragma unroll
    for (int p = 0; p < 4; ++p) { KEEP(wr[2 * p + 1]); TRP(p, wr[2 * p], wr[2 * p + 1]); }
    if (!iswo) late_unit(fr, L, ao, paddr);
    PIN();
    *(LAS u16*)(L + iaddr) = (u16)pack2(scatter4(settle<4, 0>(og, rws, ovo, oso, 512, tag)), 0.f);
    bar();
    if (iswo) late_unit(fr, L, ao, paddr);
    bar();
  } else {
    const int sv = wv;
    const rsrc_t rx = mkrs(x, NB * DM * 2);
    v4u xr[2];
    xr[0] = ld16<0>(rx, lane * 16, sv * 2048); xr[1] = ld16<0>(rx, lane * 16, sv * 2048 + 1024);
    const unsigned epoch_v = __builtin_amdgcn_raw_buffer_load_b32(rws, WS_EPOCH, 0, SC1);
    if (sv == 0) *(LAS u16*)(L + L_IN + 1024 + lane * 16 + 14) = 0;
    if (sv == 1 && lane < 12) *(LAS v4u*)(L + L_ZERO + lane * 16) = v4u{0u, 0u, 0u, 0u};
    PIN();
    bar();
    {
      float v[16];
#pragma unroll
      for (int i = 0; i < 8; ++i) { v[2 * i] = bflo(xr[i >> 2][i & 3]); v[2 * i + 1] = bfhi(xr[i >> 2][i & 3]); }
#pragma nounroll
      for (int r = isq ? 1 : 0; r < 2; ++r) ln16(v);
#pragma unroll
      for (int i = 0; i < 16; ++i)
        *(LAS u16*)(L + L_XN + sv * XN_STR + (i >> 3) * 1024 + (lane >> 3) * 128 + (i & 7) * 16 + ((lane ^ (lane >> 3) ^ (lane >> 1 & 4)) & 7) * 2) = (u16)(asu(v[i]) >> 16);
      flag_set(L + F_XN + sv * 4);
    }
    const unsigned epoch = next_epoch(__builtin_amdgcn_readfirstlane(epoch_v));
    const v4u tag = tags_of(epoch);
    const int ht = tid < 112 ? tid : 0, hb = ht / 28, hp = ht % 28, hq = ks * 28 + hp;
    const int fcol = cg * 128 + ((ks >> 1) & 3) * 32 + 8 * g + (c & 7);
    v2u bias;
    if (sv == 3) {
      bias[0] = __builtin_amdgcn_raw_buffer_load_b16(rx, fcol * 2, (ks >> 3) * DM * 2, 0);
      bias[1] = __builtin_amdgcn_raw_buffer_load_b16(mkrs(b2, DM * 2), fcol * 2, 0, 0);
    } else bias = __builtin_amdgcn_raw_buffer_load_b64(mkrs(b1, FF * 2), hp * 8, ks * 224, 0);
    PIN();
    flag_wait<2, 1>(L + F_EARLY);
    if (sv == 0) sample_put(clk(), epoch, rws, WS_T1, wg, lane);
    {
      const LAS unsigned char* q = L + L_RED + (g * 16 + sv) * E_STR + c * 16;
      f32x4 s = (*(const LAS f32x4*)q + *(const LAS f32x4*)(q + 4 * E_STR)) + (*(const LAS f32x4*)(q + 8 * E_STR) + *(const LAS f32x4*)(q + 12 * E_STR));
#pragma unroll
      for (int e = 0; e < 4; ++e) s[e] = sum_hi2(s[e]);
      if (early && lane < 16) publish4(s, tag, rws, lane * 16, (isq ? WS_QG : WS_HG) + ((ct * 2 + kh) * 4 + sv) * 256);
    }
    flag_wait<1, 4>(L + F_GO);
    if (!early) __builtin_amdgcn_s_sleep(80);
    {
      v4u qg[2];
      const int qvo = (tid >> 4) * 2048 + c * 16, qso = WS_QG + hk * 32768 + bb * 256;
      request<2>(qg, rws, qvo, qso, 1024);
      if (sv == 3) *(LAS v2u*)(L + L_REF) = v2u{sample_ref(epoch, rws, WS_T1, lane) - C1, epoch};
      const f32x4 s = settle<2, 8>(qg, rws, qvo, qso, 1024, tag);
      *(LAS v2u*)(L + L_QS + (tid >> 5) * QS_STR + (tid & 31) * 8) = v2u{pack2(s[0], s[1]), pack2(s[2], s[3])};
      flag_set(L + F_Q + sv * 4);
    }
    if (sv < 2) {
      const int hvo = (hq >> 4) * 2048 + hb * 256 + (hq & 15) * 16;
      v4u mg[2];
      request<2>(mg, rws, hvo, WS_HG, 1024);
      const f32x4 s = settle<2, 8>(mg, rws, hvo, WS_HG, 1024, tag);
      if (tid < 112) {
#pragma unroll
        for (int e = 0; e < 4; ++e) {
          const float a = rbf(rbf(s[e]) + ((e & 1) ? bfhi(bias[e >> 1]) : bflo(bias[e >> 1])));
          const float uu = 0.7978845608028654f * (a + 0.044715f * a * a * a);
          const float th = 1.f - 2.f / (1.f + __expf(2.f * uu));
          const int i = 4 * hp + e, gi = i / 7;
          *(LAS u16*)(L + L_IN + ((4 + (gi >> 2)) * 4 + hb) * 64 + (gi & 3) * 16 + (i - 7 * gi) * 2) = (u16)pack2(0.5f * a * (1.f + th), 0.f);
        }
      }
    } else if (sv == 2) {
      const int sso = WS_SG + grp * 1024;
      flag_wait<2, 2>(L + F_STAT);
      sample_put(clk(), epoch, rws, WS_T2, wg, lane);
      const f32x2 ml = *(const LAS f32x2*)(L + L_WS + lane * 8);
      const float Mc = max_hi3(ml[0]), Lc = sum_hi3(ml[1] * expd(ml[0], Mc));
      const float M1 = dpp<XOR1>(Mc), L1 = dpp<XOR1>(Lc);
      if (lane < 8 && !(lane & 1)) publish4(f32x4{Mc, Lc, M1, L1}, tag, rws, lane * 8, sso + sp * 64);
      __builtin_amdgcn_s_sleep(80);
      v4u mg[1];
      request<1>(mg, rws, lane * 16, sso, 0);
      {
        const unsigned r2 = sample_ref(epoch, rws, WS_T2, lane) - C2, r1 = *(const volatile LAS unsigned*)(L + L_REF);
        *(volatile LAS unsigned*)(L + L_REF) = r2 + min(max((int)(r2 - r1) - 10, -60), 80) * 6 / 8;
      }
      const f32x4 t = settle<1, 16>(mg, rws, lane * 16, sso, 0, tag);
      float M[2], Ls[2];
#pragma unroll
      for (int e = 0; e < 2; ++e) {
        float mm = t[2 * e]; mm = fmaxf(mm, dpp<ROR4>(mm)); M[e] = max_hi3(mm);
        float ll = t[2 * e + 1] * expd(t[2 * e], M[e]); ll += dpp<ROR4>(ll); Ls[e] = sum_hi3(ll);
      }
      const int src = (lane & 7) >> 1;
      const float Ma = __shfl(M[0], src), Mb = __shfl(M[1], src), La = __shfl(Ls[0], src), Lb = __shfl(Ls[1], src);
      *(LAS float*)(L + L_FAC + ((lane & 7) * 8 + (lane >> 3)) * 4) = expd(ml[0], (lane & 1) ? Mb : Ma) / ((lane & 1) ? Lb : La);
    }
    flag_set(L + F_MID + sv * 4);
    {
      const int hd = 2 * sv + (lane >> 5);
      flag_wait<1, 8>(L + F_MID);
      f32x4 f0 = *(const LAS f32x4*)(L + L_FAC + hd * 32), f1 = *(const LAS f32x4*)(L + L_FAC + hd * 32 + 16);
      int qa = L_RED + hd * R_STR + (lane & 31) * 16, pvo = (lane >> 3) * OG_STR + (lane & 7) * 16;
      KEEP(f0); KEEP(f1); KEEP(qa); KEEP(pvo);
      flag_wait<2, 0>(L + F_O);
      const LAS unsigned char* q = L + qa;
      f32x4 s0 = f0[0] * *(const LAS f32x4*)q, s1 = f1[0] * *(const LAS f32x4*)(q + 32 * R_STR);
#pragma unroll
      for (int p = 1; p < 4; ++p) { s0 += f0[p] * *(const LAS f32x4*)(q + p * 8 * R_STR); s1 += f1[p] * *(const LAS f32x4*)(q + (32 + p * 8) * R_STR); }
      publish4(s0 + s1, tag, rws, pvo, WS_OG + sv * 8 * OG_STR + ((hk * 4 + bb) * 16 + sp) * 128);
    }
    if (sv < 2) {
      int ra = L_RED2 + (2 * sv + (lane >> 5)) * R_STR + (lane & 31) * 16, rvo = (lane >> 3) * PG_STR + (lane & 7) * 16;
      KEEP(ra); KEEP(rvo);
      const LAS unsigned char* q = L + ra;
      asm volatile("s_barrier ; B1" ::: "memory");
      f32x4 s2 = (*(const LAS f32x4*)(q + 16 * R_STR) + *(const LAS f32x4*)(q + 20 * R_STR)) + (*(const LAS f32x4*)(q + 24 * R_STR) + *(const LAS f32x4*)(q + 28 * R_STR));
      KEEP(s2);
      asm volatile("s_barrier ; B2" ::: "memory");
      const f32x4 s = ((*(const LAS f32x4*)q + *(const LAS f32x4*)(q + 4 * R_STR)) + (*(const LAS f32x4*)(q + 8 * R_STR) + *(const LAS f32x4*)(q + 12 * R_STR))) + s2;
      publish4(s, tag, rws, rvo, WS_PG + (cg * 16 + sv * 8) * PG_STR + ks * 128);
    } else if (sv == 3 && (ks & 1) == 0) {
      const int pso = WS_PG + (cg * 16 + (ks >> 1)) * PG_STR;
      float res = bflo(bias[0]) + bflo(bias[1]);
      int ooff = fcol * 2 + (ks >> 3) * DM * 2;
      KEEP(res); KEEP(ooff);
      const rsrc_t rout = mkrs(out, NB * DM * 2);
      asm volatile("s_barrier\n\ts_barrier ; B1, B2" ::: "memory");
      __builtin_amdgcn_s_sleep(22);
      v4u pg[4];
      request<4>(pg, rws, lane * 16, pso, 1024);
      float s = scatter4(settle<4, 0>(pg, rws, lane * 16, pso, 1024, tag));
      s = (s + dpp<XOR8>(s)) + res;
      __builtin_amdgcn_raw_buffer_store_b16((u16)pack2(s, 0.f), rout, ooff, 0, SC1);
      if (wg == 0 && lane == 0) __builtin_amdgcn_raw_buffer_store_b32(epoch, rws, 0, WS_EPOCH, SC1);
    }
  }
}

extern "C" void kernel_launch(void* const* d_in, const int* in_sizes, int n_in, void* d_out, int out_size, void* d_ws, size_t ws_size, hipStream_t stream) {
  (void)hipFuncSetAttribute((const void*)fused_kernel, hipFuncAttributeMaxDynamicSharedMemorySize, LDS_BYTES);
  fused_kernel<<<NWG, NTHR, LDS_BYTES, stream>>>((const u16*)d_in[0], (const u16*)d_in[1], (const u16*)d_in[2], (const u16*)d_in[3], (const u16*)d_in[6],
                                                  (const u16*)d_in[4], (const u16*)d_in[8], (unsigned char*)d_ws, (const u16*)d_in[7], (const u16*)d_in[9], (u16*)d_out);
}
```
